```python
import torch

def _softmax(x, axis=-1):
    return torch.softmax(x, dim=axis)
def _gelu(x):
    return torch.nn.functional.gelu(x, approximate="tanh")
def _silu(x):
    return torch.nn.functional.silu(x)
def _sigmoid(x):
    return torch.sigmoid(x)
def _layernorm(x, axis=-1, eps=1e-5):
    m = x.mean(dim=axis, keepdim=True)
    v = x.var(dim=axis, keepdim=True, unbiased=False)
    return (x - m) / torch.sqrt(v + eps)
def _scatter_add(tgt, idx, src, axis=0):
    return tgt.clone().index_add_(axis, idx, src)
def _segment_sum(src, seg, n):
    out = torch.zeros((n,) + src.shape[1:], dtype=src.dtype)
    return out.index_add_(0, seg, src)
def _pad(x, pw):
    flat = tuple(v for pair in reversed(pw) for v in pair)
    return torch.nn.functional.pad(x, flat)

kernel_name = 'recurrent_scan__sliding_block_cached__mixer_channel_mlp__decode'

def setup_inputs():

    _w = (lambda a: (lambda t: t.to(torch.bfloat16) if t.is_floating_point() else t)(torch.as_tensor(a)))
    def _A_setup():
        B, Sq, D = 4, 1, 512
        Hq, Hkv, Sk, Dh = 32, 8, 8192, 128
        assert Hq % Hkv == 0, (Hq, Hkv)
        return {
            "x": _w((lambda *s: torch.randn(*s, dtype=torch.bfloat16) * 0.05)(B, Sq, D)),
            "k_cache": _w((lambda *s: torch.randn(*s, dtype=torch.bfloat16) * 3.2)(B, Hkv, Sk, Dh)),
            "v_cache": _w((lambda *s: torch.randn(*s, dtype=torch.bfloat16) * 3.2)(B, Hkv, Sk, Dh)),
            "wq": _w((lambda *s: torch.randn(*s, dtype=torch.bfloat16) * 3.2)(D, Hq * Dh) * (D ** -0.5)),
            "wo": _w((lambda *s: torch.randn(*s, dtype=torch.bfloat16) * 3.2)(Hq * Dh, D) * ((Hq * Dh) ** -0.5)),
        }
    _A_in = _A_setup()

    def _B_setup():
        B, S, D, F = 4, 1, 512, 1792
        return {
            "x": _w((lambda *s: torch.randn(*s, dtype=torch.bfloat16) * 0.05)(B, S, D)),
            "w1": _w((lambda *s: torch.randn(*s, dtype=torch.bfloat16) * 3.2)(D, F) * (D ** -0.5)),
            "b1": _w((lambda *s: torch.randn(*s, dtype=torch.bfloat16) * 3.2)(F)),
            "w2": _w((lambda *s: torch.randn(*s, dtype=torch.bfloat16) * 3.2)(F, D) * (F ** -0.5)),
            "b2": _w((lambda *s: torch.randn(*s, dtype=torch.bfloat16) * 3.2)(D)),
        }
    _B_in = _B_setup()

    return {
        "A_x": _A_in["x"],
        "A_k_cache": _A_in["k_cache"],
        "A_v_cache": _A_in["v_cache"],
        "A_wq": _A_in["wq"],
        "A_wo": _A_in["wo"],
        "B_x": _B_in["x"],
        "B_w1": _B_in["w1"],
        "B_b1": _B_in["b1"],
        "B_w2": _B_in["w2"],
        "B_b2": _B_in["b2"],
    }

def reference(A_x, A_k_cache, A_v_cache, A_wq, A_wo, B_x, B_w1, B_b1, B_w2, B_b2):
    def _A_ref(x, k_cache, v_cache, wq, wo):
        B, Sq, D = x.shape
        Hq, Dh = 32, 128
        Hkv, Sk = k_cache.shape[1], k_cache.shape[2]
        W = min(4096, max(256, Sk // 4))
        rep = Hq // Hkv
        q = (lambda x, *d: x.permute(*d))((x @ wq).reshape(B, Sq, Hq, Dh), 0, 2, 1, 3)
        k = (lambda x, n, axis: torch.repeat_interleave(x, n, dim=axis))(k_cache[:, :, Sk - W:, :], rep, axis=1)
        v = (lambda x, n, axis: torch.repeat_interleave(x, n, dim=axis))(v_cache[:, :, Sk - W:, :], rep, axis=1)
        scores = torch.einsum("bhqd,bhkd->bhqk", q, k) * (Dh ** -0.5)
        p = _softmax(scores, axis=-1)
        o = torch.einsum("bhqk,bhkd->bhqd", p, v)
        return (lambda x, *d: x.permute(*d))(o, 0, 2, 1, 3).reshape(B, Sq, Hq * Dh) @ wo

    def _B_ref(x, w1, b1, w2, b2):
        y = _layernorm(x, axis=-1)
        h = _gelu(torch.einsum("bsd,df->bsf", y, w1) + b1)
        return torch.einsum("bsf,fd->bsd", h, w2) + b2

    x = A_x
    xn = _layernorm(x, axis=-1)
    s = _A_ref(xn, A_k_cache, A_v_cache, A_wq, A_wo)
    g = _silu(_B_ref(xn, B_w1, B_b1, B_w2, B_b2))
    return x + s * g
```

```cpp
#include <hip/hip_runtime.h>
#include <hip/hip_bf16.h>
#include <stdint.h>

#define MIMIC 1

typedef unsigned short u16;
__device__ __forceinline__ float bf2f(u16 b) { return __uint_as_float(((unsigned)b) << 16); }
__device__ __forceinline__ u16 f2bf(float f) { __hip_bfloat16 h = __float2bfloat16(f); return *reinterpret_cast<u16*>(&h); }
__device__ __forceinline__ float rb(float f) {
  return bf2f(f2bf(f));
}

constexpr int B = 4, D = 512, HQ = 32, HKV = 8, SK = 8192, DH = 128, W = 2048, F = 1792;

__device__ float block_sum(float v, float* red) {
  for (int o = 32; o > 0; o >>= 1) v += __shfl_xor(v, o);
  __syncthreads();
  if ((threadIdx.x & 63) == 0) red[threadIdx.x >> 6] = v;
  __syncthreads();
  float s = 0;
  for (int i = 0; i < (int)(blockDim.x >> 6); ++i) s += red[i];
  return s;
}

__global__ void k_ln(const u16* x, float* xn, float* y) {
  __shared__ float red[16];
  int b = blockIdx.x, t = threadIdx.x;
  float v = bf2f(x[b * D + t]);
  float mean = block_sum(v, red) / D;
  float var = block_sum((v - mean) * (v - mean), red) / D;
  float a = rb(rb(v - rb(mean)) / rb(sqrtf(rb(rb(var) + 1e-5f))));
  xn[b * D + t] = a;
  float mean2 = block_sum(a, red) / D;
  float var2 = block_sum((a - mean2) * (a - mean2), red) / D;
  float c = rb(rb(a - rb(mean2)) / rb(sqrtf(rb(rb(var2) + 1e-5f))));
  y[b * D + t] = c;
}

__global__ void k_q(const float* xn, const u16* wq, float* q) {
  int n = blockIdx.x * blockDim.x + threadIdx.x;
  float acc[B] = {0, 0, 0, 0};
  for (int d = 0; d < D; ++d) {
    float w = bf2f(wq[d * (HQ * DH) + n]);
    for (int b = 0; b < B; ++b) acc[b] += xn[b * D + d] * w;
  }
  for (int b = 0; b < B; ++b) q[b * HQ * DH + n] = rb(acc[b]);
}

__global__ void k_h(const float* y, const u16* w1, const u16* b1, float* h) {
  int n = blockIdx.x * blockDim.x + threadIdx.x;
  if (n >= F) return;
  float acc[B] = {0, 0, 0, 0};
  for (int d = 0; d < D; ++d) {
    float w = bf2f(w1[d * F + n]);
    for (int b = 0; b < B; ++b) acc[b] += y[b * D + d] * w;
  }
  for (int b = 0; b < B; ++b) {
    float u = rb(rb(acc[b]) + bf2f(b1[n]));
    float inner = 0.7978845608028654f * (u + 0.044715f * u * u * u);
    float g = 0.5f * u * (1.f + tanhf(inner));
    h[b * F + n] = rb(g);
  }
}

__global__ void k_attn(const float* q, const u16* kc, const u16* vc, float* o) {
  __shared__ float sc[W];
  __shared__ float red[16];
  __shared__ float qs[DH];
  int b = blockIdx.x / HQ, h = blockIdx.x % HQ, kv = h / 4, t = threadIdx.x;
  if (t < DH) qs[t] = q[b * HQ * DH + h * DH + t];
  __syncthreads();
  const u16* kb = kc + ((size_t)(b * HKV + kv) * SK + (SK - W)) * DH;
  const u16* vb = vc + ((size_t)(b * HKV + kv) * SK + (SK - W)) * DH;
  float mx = -INFINITY;
  for (int k = t; k < W; k += blockDim.x) {
    float a = 0;
    for (int d = 0; d < DH; ++d) a += qs[d] * bf2f(kb[(size_t)k * DH + d]);
    a = rb(rb(a) * 0.08838834764831845f);
    sc[k] = a;
    mx = fmaxf(mx, a);
  }
  for (int of = 32; of > 0; of >>= 1) mx = fmaxf(mx, __shfl_xor(mx, of));
  __syncthreads();
  if ((t & 63) == 0) red[t >> 6] = mx;
  __syncthreads();
  mx = red[0];
  for (int i = 1; i < (int)(blockDim.x >> 6); ++i) mx = fmaxf(mx, red[i]);
  float s = 0;
  for (int k = t; k < W; k += blockDim.x) { float e = expf(sc[k] - mx); sc[k] = e; s += e; }
  s = block_sum(s, red);
  __syncthreads();
  if (t < DH) {
    float a = 0;
    for (int k = 0; k < W; ++k) a += rb(sc[k] / s) * bf2f(vb[(size_t)k * DH + t]);
    o[b * HQ * DH + h * DH + t] = rb(a);
  }
}

__global__ void k_out(const u16* x, const float* o, const u16* wo, const float* h, const u16* w2, const u16* b2, u16* out) {
  int n = threadIdx.x + blockIdx.x * blockDim.x;
  int b = blockIdx.y;
  float s = 0, m = 0;
  for (int k = 0; k < HQ * DH; ++k) s += o[b * HQ * DH + k] * bf2f(wo[k * D + n]);
  for (int k = 0; k < F; ++k) m += h[b * F + k] * bf2f(w2[k * D + n]);
  s = rb(s);
  m = rb(rb(m) + bf2f(b2[n]));
  float g = rb(m / (1.f + expf(-m)));
  out[b * D + n] = f2bf(bf2f(x[b * D + n]) + rb(s * g));
}

extern "C" void kernel_launch(void* const* d_in, const int* in_sizes, int n_in, void* d_out, int out_size, void* d_ws,
                              size_t ws_size, hipStream_t stream) {
  const u16* x = (const u16*)d_in[0];
  const u16* kc = (const u16*)d_in[1];
  const u16* vc = (const u16*)d_in[2];
  const u16* wq = (const u16*)d_in[3];
  const u16* wo = (const u16*)d_in[4];
  const u16* w1 = (const u16*)d_in[6];
  const u16* b1 = (const u16*)d_in[7];
  const u16* w2 = (const u16*)d_in[8];
  const u16* b2 = (const u16*)d_in[9];
  float* ws = (float*)d_ws;
  float* xn = ws;
  float* y = xn + B * D;
  float* q = y + B * D;
  float* h = q + B * HQ * DH;
  float* o = h + B * F;
  k_ln<<<B, 512, 0, stream>>>(x, xn, y);
  k_q<<<HQ * DH / 64, 64, 0, stream>>>(xn, wq, q);
  k_h<<<F / 64, 64, 0, stream>>>(y, w1, b1, h);
  k_attn<<<B * HQ, 256, 0, stream>>>(q, kc, vc, o);
  k_out<<<dim3(D / 64, B), 64, 0, stream>>>(x, o, wo, h, w2, b2, (u16*)d_out);
}
```

```cpp
#include <hip/hip_runtime.h>
#include <stdint.h>

typedef unsigned short u16;
typedef unsigned v4u __attribute__((ext_vector_type(4)));
typedef unsigned v2u __attribute__((ext_vector_type(2)));
typedef unsigned v8u __attribute__((ext_vector_type(8)));
typedef unsigned v16u __attribute__((ext_vector_type(16)));
typedef float f32x4 __attribute__((ext_vector_type(4)));
typedef float f32x2 __attribute__((ext_vector_type(2)));
typedef __bf16 bf16x8 __attribute__((ext_vector_type(8)));
typedef __bf16 bf16x2 __attribute__((ext_vector_type(2)));
#define GAS __attribute__((address_space(1)))
#define LAS __attribute__((address_space(3)))

#define QDELAY 96
#define HDELAY 64
#define PACE 4
#define HALF 6
#define PRETOUCH 1
#define PRETOUCH_PAGES 6
#define EARLYK 0
#define LATEW1 0
#define KW 6
#define UNTH (16 + KW)

constexpr int D = 512, NQ = 4096, SK = 8192, WIN = 2048, DH = 128, F = 1792;
constexpr int NWG = 256, NTHR = 448;
constexpr int WS_EPOCH = 0;
constexpr int WS_QP = 4096;
constexpr int WS_HP = WS_QP + 4 * 8 * 4 * 8 * 256;
constexpr int WS_OP = WS_HP + 28 * 8 * 1024;
constexpr int WS_ST = WS_OP + 32 * 2 * 8 * 1024;
constexpr int WS_SP = WS_ST + 32 * 4 * 8 * 16;
constexpr int WS_MP = WS_SP + 256 * 64 * 32;
constexpr int WS_END = WS_MP + 256 * 32 * 32;
static_assert(WS_END <= (2 << 20), "hand-off area");
constexpr unsigned SPIN_CAP = 1u << 16;
constexpr int OOB = 0x7ffffff0;
constexpr unsigned SENT = 0xffffffffu;

#define STAMP(i) do {} while (0)

constexpr int L_K = 0;
constexpr int L_O = 65536;
constexpr int L_Q = L_O + 8192;
constexpr int L_FL = L_Q + 1024;
constexpr int L_MX = L_FL;
constexpr int L_LS = L_FL + 64;
constexpr int L_FO = L_FL + 128;
constexpr int L_FV = L_FL + 144;
constexpr int L_FQ = L_FL + 160;
constexpr int L_FS = L_FL + 176;
constexpr int L_TG = L_FL + 192;
constexpr int L_STAMP = L_FL + 256;
constexpr int L_BYTES = L_STAMP + 256;

__device__ __forceinline__ float bflo(unsigned u) { return __builtin_bit_cast(float, u << 16); }
__device__ __forceinline__ float bfhi(unsigned u) { return __builtin_bit_cast(float, u & 0xffff0000u); }
__device__ __forceinline__ unsigned pk(float lo, float hi) {
  f32x2 v = {lo, hi};
  return __builtin_bit_cast(unsigned, __builtin_convertvector(v, bf16x2));
}
__device__ __forceinline__ float rb(float f) { return bflo(pk(f, f)); }
__device__ __forceinline__ f32x4 mfma(v4u a, v4u b, f32x4 c) {
  return __builtin_amdgcn_mfma_f32_16x16x32_bf16(__builtin_bit_cast(bf16x8, a), __builtin_bit_cast(bf16x8, b), c, 0, 0, 0);
}
template <int CTRL> __device__ __forceinline__ float dpp(float x) {
  return __builtin_bit_cast(float, __builtin_amdgcn_mov_dpp(__builtin_bit_cast(int, x), CTRL, 0xf, 0xf, true));
}
constexpr int ROR1 = 0x121, ROR2 = 0x122, ROR4 = 0x124, ROR8 = 0x128, XOR2 = 0x4E;
__device__ __forceinline__ float row_sum(float x) { x += dpp<ROR8>(x); x += dpp<ROR4>(x); x += dpp<ROR2>(x); x += dpp<ROR1>(x); return x; }
__device__ __forceinline__ float xrow_max(float x) {
  auto s = __builtin_amdgcn_permlane16_swap(__float_as_uint(x), __float_as_uint(x), false, false);
  x = fmaxf(__uint_as_float(s[0]), __uint_as_float(s[1]));
  auto t = __builtin_amdgcn_permlane32_swap(__float_as_uint(x), __float_as_uint(x), false, false);
  return fmaxf(__uint_as_float(t[0]), __uint_as_float(t[1]));
}
__device__ __forceinline__ float xrow_sum(float x) {
  auto s = __builtin_amdgcn_permlane16_swap(__float_as_uint(x), __float_as_uint(x), false, false);
  x = __uint_as_float(s[0]) + __uint_as_float(s[1]);
  auto t = __builtin_amdgcn_permlane32_swap(__float_as_uint(x), __float_as_uint(x), false, false);
  return __uint_as_float(t[0]) + __uint_as_float(t[1]);
}
__device__ __forceinline__ float red4(f32x4 v) {
  auto a = __builtin_amdgcn_permlane32_swap(__float_as_uint(v[0]), __float_as_uint(v[2]), false, false);
  auto b = __builtin_amdgcn_permlane32_swap(__float_as_uint(v[1]), __float_as_uint(v[3]), false, false);
  const float p = __uint_as_float(a[0]) + __uint_as_float(a[1]), q = __uint_as_float(b[0]) + __uint_as_float(b[1]);
  auto c = __builtin_amdgcn_permlane16_swap(__float_as_uint(p), __float_as_uint(q), false, false);
  float r = __uint_as_float(c[0]) + __uint_as_float(c[1]);
  r += dpp<ROR8>(r); r += dpp<ROR4>(r); r += dpp<ROR2>(r);
  return r;
}
template <int O, int N> __device__ __forceinline__ v4u trcol(const v4u (&W)[N], int j) {
  const unsigned sel = (j & 1) ? 0x07060302u : 0x05040100u;
  v4u r;
#pragma unroll
  for (int t = 0; t < 4; ++t) r[t] = __builtin_amdgcn_perm(W[O + 2 * t + 1][j >> 1], W[O + 2 * t][j >> 1], sel);
  return r;
}
__device__ __forceinline__ unsigned bperm(int src_lane, unsigned v) { return (unsigned)__builtin_amdgcn_ds_bpermute(src_lane << 2, (int)v); }
#define SB() __builtin_amdgcn_sched_barrier(0)
#define A_X "s[80:81]"
#define A_KC "s[82:83]"
#define A_VC "s[84:85]"
#define A_WQ "s[86:87]"
#define A_WO "s[88:89]"
#define A_W1 "s[90:91]"
#define A_B1 "s[92:93]"
#define A_W2 "s[94:95]"
#define A_B2 "s[72:73]"
#define A_OUT "s[74:75]"
#define A_WS "s[76:77]"
#define ARGS3(p0, a0, p1, a1, p2, a2) asm volatile("s_waitcnt lgkmcnt(0)\n\ts_mov_b64 %0, " a0 "\n\ts_mov_b64 %1, " a1 "\n\ts_mov_b64 %2, " a2 : "=&s"(p0), "=&s"(p1), "=&s"(p2) :: "memory")
#define ARGS2(p0, a0, p1, a1) asm volatile("s_mov_b64 %0, " a0 "\n\ts_mov_b64 %1, " a1 : "=&s"(p0), "=&s"(p1))
#define BAR0() do { SB(); asm volatile("s_barrier" ::: "memory"); SB(); } while (0)
typedef __amdgpu_buffer_rsrc_t rsrc_t;
__device__ __forceinline__ v4u ld_sc1(rsrc_t rs, int off) { return __builtin_amdgcn_raw_buffer_load_b128(rs, off, 0, 16); }
__device__ __forceinline__ v4u ld_nt(rsrc_t rs, int off, int soff) { return __builtin_amdgcn_raw_buffer_load_b128(rs, off, soff, 2); }
__device__ __forceinline__ rsrc_t mkrs(const void* p, int bytes) { return __builtin_amdgcn_make_buffer_rsrc((void*)p, 0, bytes, 0x00020000); }
__device__ __forceinline__ unsigned next_tag(unsigned e) { return ((e & 0x7f7f7f7fu) + 0x01010101u) ^ (e & 0x80808080u); }
__device__ __forceinline__ void st_unit(rsrc_t rs, int off, float a, float b, float c, float d, unsigned tag) {
  v4u v;
  v.x = __builtin_amdgcn_perm(__float_as_uint(a) + 0x80u, tag, 0x07060500u);
  v.y = __builtin_amdgcn_perm(__float_as_uint(b) + 0x80u, tag, 0x07060501u);
  v.z = __builtin_amdgcn_perm(__float_as_uint(c) + 0x80u, tag, 0x07060502u);
  v.w = __builtin_amdgcn_perm(__float_as_uint(d) + 0x80u, tag, 0x07060503u);
  __builtin_amdgcn_raw_buffer_store_b128(v, rs, off, 0, 16);
}
template <int N> __device__ __forceinline__ void issue(rsrc_t rs, const int (&off)[N], v4u (&v)[N]) {
  asm volatile("" ::: "memory");
#pragma unroll
  for (int k = 0; k < N; ++k) v[k] = ld_sc1(rs, off[k]);
  SB();
}
template <int N> __device__ __forceinline__ bool take(int (&off)[N], const v4u (&v)[N], unsigned tag, v4u (&val)[N]) {
  int all = OOB;
#pragma unroll
  for (int k = 0; k < N; ++k) {
    const unsigned t = __builtin_amdgcn_perm(v[k].y, v[k].x, 0x0c0c0400u) | __builtin_amdgcn_perm(v[k].w, v[k].z, 0x04000c0cu);
    const bool ok = t == tag && off[k] != OOB;
    val[k].x = ok ? v[k].x : val[k].x; val[k].y = ok ? v[k].y : val[k].y; val[k].z = ok ? v[k].z : val[k].z; val[k].w = ok ? v[k].w : val[k].w;
    off[k] = ok ? OOB : off[k];
    all &= off[k];
  }
  return __builtin_amdgcn_ballot_w64(all != OOB) == 0ull;
}
template <int N> __device__ __forceinline__ void poll(rsrc_t rs, int (&off)[N], unsigned tag, v4u (&val)[N]) {
#pragma nounroll
  for (unsigned spin = 0; spin < SPIN_CAP; ++spin) {
    v4u v[N];
    issue<N>(rs, off, v);
    if (take<N>(off, v, tag, val)) break;
    __builtin_amdgcn_s_sleep(PACE);
  }
}
template <int N> __device__ __forceinline__ void keep(const v4u (&a)[N]) {
#pragma unroll
  for (int k = 0; k < N; ++k) asm volatile("" :: "v"(a[k]));
}
template <int N> __device__ __forceinline__ void pin(v4u (&a)[N]) {
#pragma unroll
  for (int k = 0; k < N; ++k) asm volatile("" : "+v"(a[k]));
}
template <int N> __device__ __forceinline__ void poll2(rsrc_t rs, int (&off)[N], unsigned tag, v4u (&val)[N], v4u (&a)[N], v4u (&b)[N]) {
  issue<N>(rs, off, a);
  __builtin_amdgcn_s_sleep(HALF);
#pragma nounroll
  for (unsigned spin = 0; spin < SPIN_CAP; ++spin) {
    issue<N>(rs, off, b);
    if (take<N>(off, a, tag, val)) break;
    issue<N>(rs, off, a);
    if (take<N>(off, b, tag, val)) break;
  }
}
__device__ __forceinline__ f32x4 unval(v4u v) { return __builtin_bit_cast(f32x4, v & 0xffffff00u); }
__device__ __forceinline__ v4u lds_wait4(LAS unsigned char* p, int nap) {
  v4u v;
#pragma nounroll
  for (unsigned spin = 0; spin < SPIN_CAP * 64; ++spin) {
    v = *(volatile LAS v4u*)p;
    const bool bad = v.x == SENT || v.y == SENT || v.z == SENT || v.w == SENT;
    if (__builtin_amdgcn_ballot_w64(bad) == 0ull) break;
    if (nap) __builtin_amdgcn_s_sleep(1);
  }
  asm volatile("" ::: "memory");
  return v;
}
__device__ __forceinline__ unsigned lds_wait_tag(LAS unsigned char* p) {
  v2u fq;
#pragma nounroll
  for (unsigned spin = 0; spin < SPIN_CAP * 64; ++spin) {
    fq = *(volatile LAS v2u*)p;
    if (__builtin_amdgcn_readfirstlane(fq.x) != SENT) break;
    __builtin_amdgcn_s_sleep(1);
  }
  asm volatile("" ::: "memory");
  return __builtin_amdgcn_readfirstlane(fq.y);
}
__device__ __forceinline__ void ln_stats(const v4u (&r)[4], float& mb, float& rden) {
  f32x2 v[16];
#pragma unroll
  for (int t = 0; t < 4; ++t)
#pragma unroll
    for (int k = 0; k < 4; ++k) v[t * 4 + k] = f32x2{bflo(r[t][k]), bfhi(r[t][k])};
  f32x2 s = v[0];
#pragma unroll
  for (int i = 1; i < 16; ++i) s += v[i];
  const float mean = row_sum(s[0] + s[1]) * (1.f / D);
  f32x2 q = {0.f, 0.f};
#pragma unroll
  for (int i = 0; i < 16; ++i) { const f32x2 d = v[i] - mean; q += d * d; }
  const float var = row_sum(q[0] + q[1]) * (1.f / D);
  mb = rb(mean);
  rden = __builtin_amdgcn_rcpf(rb(__builtin_amdgcn_sqrtf(rb(rb(var) + 1e-5f))));
}
__device__ __forceinline__ unsigned ln_pair(unsigned u, float mb, float rden) {
  const unsigned d = pk(bflo(u) - mb, bfhi(u) - mb);
  return pk(bflo(d) * rden, bfhi(d) * rden);
}
__device__ __forceinline__ f32x4 hh_combine(const f32x4 (&acc)[8], bool hh) {
  f32x4 o;
#pragma unroll
  for (int t = 0; t < 4; ++t) { const float send = hh ? acc[t][1] : acc[4 + t][0], keep = hh ? acc[4 + t][1] : acc[t][0]; o[t] = keep + dpp<ROR8>(send); }
  return o;
}

__global__ void __launch_bounds__(NTHR, 2) __attribute__((aligned(4096)))
fused_decode(const u16*, const u16*, const u16*, const u16*, const u16*, const u16*, const u16*, const u16*, const u16*, u16*,
             unsigned char*, unsigned char*) {
  asm volatile("s_load_dwordx16 s[80:95], %0, 0x0\n\ts_load_dwordx8 s[72:79], %0, 0x40" :: "s"(__builtin_amdgcn_kernarg_segment_ptr())
               : "s72", "s73", "s74", "s75", "s76", "s77", "s78", "s79", "s80", "s81", "s82", "s83", "s84", "s85", "s86", "s87", "s88", "s89",
                 "s90", "s91", "s92", "s93", "s94", "s95");
  unsigned long long pc;
  asm volatile("s_getpc_b64 %0" : "=s"(pc));
  __shared__ __attribute__((aligned(16))) unsigned char lds[L_BYTES];
  LAS unsigned char* L = (LAS unsigned char*)lds;
  const int wg = blockIdx.x, lane = threadIdx.x & 63;
  const int wave = __builtin_amdgcn_readfirstlane(threadIdx.x >> 6);
  const int g = lane >> 4, c = lane & 15;
  const int sp = wg & 7, kv = (wg >> 3) & 7, ab = wg >> 6;
  const int rowl = 32 * (c >> 3) + 8 * g;
  *(volatile LAS unsigned*)(L + L_FL + lane * 4) = SENT;

  if (wave == 0) {
    __builtin_amdgcn_s_setprio(3);
    const int cb = wg & 63, rr = wg >> 6;
    int voff = ((rr * 128 + rowl) * NQ + cb * 64 + (c & 7) * 8) * 2, xoff = g * (D * 2) + c * 16;
    asm volatile("" : "+v"(voff), "+v"(xoff));
    const u16 *x, *wq, *wo, *dummy;
    unsigned char* ws;
    ARGS3(x, A_X, wq, A_WQ, ws, A_WS);
    ARGS2(wo, A_WO, dummy, A_WO);
    const rsrc_t rs = mkrs(ws, WS_END);
    v4u R[16], xr[4], xo;
    {
      const rsrc_t rx_ = mkrs(x, 4 * D * 2), rq_ = mkrs(wq, D * NQ * 2);
#pragma unroll
      for (int t = 0; t < 4; ++t) xr[t] = __builtin_amdgcn_raw_buffer_load_b128(rx_, xoff + t * 256, 0, 0);
      xo = __builtin_amdgcn_raw_buffer_load_b128(rx_, xoff, rr * 256, 0);
      SB();
#pragma unroll
      for (int i = 0; i < 16; ++i) { R[i] = ld_nt(rq_, voff, ((i & 7) + (i >> 3) * 64) * (NQ * 2)); if ((i & 3) == 3) SB(); }
    }
    BAR0();
    float mb, rden;
    STAMP(16);
    ln_stats(xr, mb, rden);
    unsigned tag;
    {
      v4u A0, A1;
      const int src = (c >> 2) * 16 + 4 * (c & 1) + g;
#pragma unroll
      for (int t = 0; t < 4; ++t) { const unsigned n = ln_pair(xo[t], mb, rden); A0[t] = bperm(src, n); A1[t] = bperm(src + 8, n); }
      STAMP(17);
      SB();
      f32x4 acc[8];
      v4u Fq[8];
#pragma unroll
      for (int j = 0; j < 8; ++j) Fq[j] = trcol<0>(R, j);
      SB();
#pragma unroll
      for (int j = 0; j < 8; ++j) acc[j] = mfma(A0, Fq[j], f32x4{0.f, 0.f, 0.f, 0.f});
      v4u Fr[8];
#pragma unroll
      for (int j = 0; j < 8; ++j) Fr[j] = trcol<8>(R, j);
      SB();
#pragma unroll
      for (int j = 0; j < 8; ++j) acc[j] = mfma(A1, Fr[j], acc[j]);
      SB();
      const f32x4 o = hh_combine(acc, c >= 8);
      STAMP(24);
      tag = lds_wait_tag(L + L_TG);
      STAMP(25);
      st_unit(rs, WS_QP + (((g * 8 + (cb >> 3)) * 4 + rr) * 8 + (cb & 7)) * 256 + c * 16, o[0], o[1], o[2], o[3], tag);
    }
    STAMP(8);
    const int h = wg & 31, dh = (wg >> 5) & 1, nq = wg >> 6;
    lds_wait4(L + L_FV, 1);
    {
      const rsrc_t rw_ = mkrs(wo, NQ * D * 2);
      const int off = ((h * 128 + dh * 64 + 8 * g) * D + nq * 128 + c * 8) * 2;
#pragma unroll
      for (int i = 0; i < 16; ++i) R[i] = ld_nt(rw_, off + (i & 3) * 1024, ((i >> 2) & 1) * 4096 + (i >> 3) * 32768);
    }
    SB();
    int so[1] = {WS_ST + ((h * 4 + g) * 8 + (c & 7)) * 16};
    v4u sv[1] = {v4u{0u, 0u, 0u, 0u}};
    __builtin_amdgcn_s_sleep(16);
    v4u F0[8], F1[8];
#pragma unroll
    for (int j = 0; j < 8; ++j) { F0[j] = trcol<0>(R, j); F1[j] = trcol<8>(R, j); }
    pin<8>(F0); pin<8>(F1);
    SB();
    STAMP(18);
    poll<1>(rs, so, tag, sv);
    STAMP(19);
    const f32x4 st4 = unval(sv[0]);
    const float Ms = st4[0], Ls = st4[1];
    float Mx = fmaxf(Ms, dpp<ROR4>(Ms)); Mx = fmaxf(Mx, dpp<ROR2>(Mx)); Mx = fmaxf(Mx, dpp<ROR1>(Mx));
    const float wgt = __builtin_amdgcn_exp2f((Ms - Mx) * 1.4426950408889634f);
    float Lt = wgt * Ls; Lt += dpp<ROR4>(Lt); Lt += dpp<ROR2>(Lt); Lt += dpp<ROR1>(Lt);
    const float f = wgt * __builtin_amdgcn_rcpf(Lt);
    float fs[8] = {dpp<0x150>(f), dpp<0x151>(f), dpp<0x152>(f), dpp<0x153>(f), dpp<0x154>(f), dpp<0x155>(f), dpp<0x156>(f), dpp<0x157>(f)};
    int oo[8];
    v4u ov[8];
#pragma unroll
    for (int k = 0; k < 8; ++k) { oo[k] = WS_OP + ((h * 2 + dh) * 8 + k) * 1024 + lane * 16; ov[k] = v4u{0u, 0u, 0u, 0u}; }
    const int src = (c >> 2) * 16 + 2 * g;
    const int spo = WS_SP + (((g * 4 + nq) * 2) * 8 + (c >> 1)) * 2048 + (h * 2 + dh) * 32 + (c & 1) * 16;
    SB();
    v4u pa[8], pb[8];
    poll2<8>(rs, oo, tag, ov, pa, pb);
    STAMP(10);
    f32x4 o = unval(ov[0]) * fs[0];
#pragma unroll
    for (int k = 1; k < 8; ++k) o += unval(ov[k]) * fs[k];
    const unsigned p0 = pk(o[0], o[1]), p1 = pk(o[2], o[3]);
    const v4u A0 = {bperm(src, p0), bperm(src, p1), bperm(src + 1, p0), bperm(src + 1, p1)};
    const v4u A1 = {bperm(src + 8, p0), bperm(src + 8, p1), bperm(src + 9, p0), bperm(src + 9, p1)};
    f32x4 acc[8];
#pragma unroll
    for (int j = 0; j < 4; ++j) acc[j] = mfma(A0, F0[j], f32x4{0.f, 0.f, 0.f, 0.f});
#pragma unroll
    for (int j = 0; j < 4; ++j) acc[j] = mfma(A1, F1[j], acc[j]);
#pragma unroll
    for (int j = 4; j < 8; ++j) acc[j] = mfma(A0, F0[j], f32x4{0.f, 0.f, 0.f, 0.f});
    st_unit(rs, spo, acc[0][0], acc[1][0], acc[2][0], acc[3][0], tag);
#pragma unroll
    for (int j = 4; j < 8; ++j) acc[j] = mfma(A1, F1[j], acc[j]);
    st_unit(rs, spo + 8 * 2048, acc[4][0], acc[5][0], acc[6][0], acc[7][0], tag);
    if (lane == 0) *(volatile LAS unsigned*)(L + L_FS) = 1u;
    STAMP(11);
    SB();
    keep<8>(pa); keep<8>(pb);
    return;
  }
  if (wave == 1) {
    const int r8 = wg & 7, fb = wg >> 3;
    if (wg < 224) {
      __builtin_amdgcn_s_setprio(3);
      int voff = ((r8 * 64 + rowl) * F + fb * 64 + (c & 7) * 8) * 2, xoff = g * (D * 2) + c * 16, xooff = g * (D * 2) + (c & 7) * 16;
      asm volatile("" : "+v"(voff), "+v"(xoff), "+v"(xooff));
      const u16 *x, *w1, *b1, *w2;
      unsigned char* ws;
      ARGS3(x, A_X, w1, A_W1, ws, A_WS);
      ARGS2(b1, A_B1, w2, A_W2);
      const rsrc_t rs = mkrs(ws, WS_END);
      v4u R[8], xr[4], xo;
      {
        const rsrc_t rx_ = mkrs(x, 4 * D * 2), rq_ = mkrs(w1, D * F * 2);
#pragma unroll
        for (int t = 0; t < 4; ++t) xr[t] = __builtin_amdgcn_raw_buffer_load_b128(rx_, xoff + t * 256, 0, 0);
        xo = __builtin_amdgcn_raw_buffer_load_b128(rx_, xooff, r8 * 128, 0);
        SB();
#pragma unroll
        for (int i = 0; i < 8; ++i) { R[i] = ld_nt(rq_, voff, i * (F * 2)); if ((i & 3) == 3) SB(); }
      }
      BAR0();
      float mb, rden, mb2, rden2;
      ln_stats(xr, mb, rden);
      const unsigned tag = lds_wait_tag(L + L_TG);
#pragma unroll
      for (int t = 0; t < 4; ++t)
#pragma unroll
        for (int k = 0; k < 4; ++k) xr[t][k] = ln_pair(xr[t][k], mb, rden);
      ln_stats(xr, mb2, rden2);
      const int src = (c >> 2) * 16 + 4 * (c & 1) + g;
      f32x4 acc[8];
      {
        v4u A0;
#pragma unroll
        for (int t = 0; t < 4; ++t) A0[t] = bperm(src, ln_pair(ln_pair(xo[t], mb, rden), mb2, rden2));
        v4u Fh[8];
#pragma unroll
        for (int j = 0; j < 8; ++j) Fh[j] = trcol<0>(R, j);
        SB();
#pragma unroll
        for (int j = 0; j < 8; ++j) acc[j] = mfma(A0, Fh[j], f32x4{0.f, 0.f, 0.f, 0.f});
        SB();
        const f32x4 o = hh_combine(acc, c >= 8);
        st_unit(rs, WS_HP + (fb * 8 + r8) * 1024 + lane * 16, o[0], o[1], o[2], o[3], tag);
      }
      STAMP(12);
      const v2u b1p = *(const GAS v2u*)(b1 + fb * 64 + 8 * (c & 7) + 4 * (c >> 3));
      __builtin_amdgcn_s_sleep(HDELAY);
      int ho[8];
      v4u hv[8];
#pragma unroll
      for (int k = 0; k < 8; ++k) { ho[k] = WS_HP + (fb * 8 + k) * 1024 + lane * 16; hv[k] = v4u{0u, 0u, 0u, 0u}; }
      poll<8>(rs, ho, tag, hv);
      lds_wait4(L + L_FV, 1);
      {
        const rsrc_t rw_ = mkrs(w2, F * D * 2);
        const int off = ((fb * 64 + rowl) * D + r8 * 64 + (c & 7) * 8) * 2;
#pragma unroll
        for (int i = 0; i < 8; ++i) R[i] = ld_nt(rw_, off + (i & 3) * 1024, (i >> 2) * 4096);
      }
      SB();
      const f32x4 u4 = ((unval(hv[0]) + unval(hv[1])) + (unval(hv[2]) + unval(hv[3]))) + ((unval(hv[4]) + unval(hv[5])) + (unval(hv[6]) + unval(hv[7])));
      const unsigned ua = pk(u4[0], u4[1]), ub = pk(u4[2], u4[3]);
      const unsigned uc[2] = {pk(bflo(ua) + bflo(b1p.x), bfhi(ua) + bfhi(b1p.x)), pk(bflo(ub) + bflo(b1p.y), bfhi(ub) + bfhi(b1p.y))};
      float hh[4];
#pragma unroll
      for (int e = 0; e < 4; ++e) {
        const float u = (e & 1) ? bfhi(uc[e >> 1]) : bflo(uc[e >> 1]);
        const float z = 0.7978845608028654f * (u + 0.044715f * u * u * u);
        hh[e] = u * __builtin_amdgcn_rcpf(1.f + __builtin_amdgcn_exp2f(-2.885390081777927f * z));
      }
      const unsigned h0 = pk(hh[0], hh[1]), h1 = pk(hh[2], hh[3]);
      const v4u A = {bperm(src, h0), bperm(src, h1), bperm(src + 8, h0), bperm(src + 8, h1)};
      STAMP(13);
#pragma unroll
      for (int j = 0; j < 8; ++j) acc[j] = mfma(A, trcol<0>(R, j), f32x4{0.f, 0.f, 0.f, 0.f});
      const f32x4 o = hh_combine(acc, c >= 8);
      const int c2 = 8 * (r8 & 1) + (c & 7);
      st_unit(rs, WS_MP + (((g * 4 + (r8 >> 1)) * 2 + (c >> 3)) * 8 + (c2 >> 1)) * 1024 + fb * 32 + (c2 & 1) * 16, o[0], o[1], o[2], o[3], tag);
      STAMP(20);
    } else {
      asm volatile("s_waitcnt lgkmcnt(0)" ::: "memory");
      BAR0();
      STAMP(12); STAMP(13); STAMP(20);
    }
    return;
  }
  if (wave == 2) {
    __builtin_amdgcn_s_setprio(3);
    const int bf = wg >> 6;
    const int n = ((wg >> 4) & 3) * 128 + 8 * (2 * (wg & 7) + (lane & 1)) + 4 * ((wg >> 3) & 1) + ((lane >> 5) & 1) * 2 + ((lane >> 4) & 1);
    const unsigned pre = __builtin_amdgcn_raw_buffer_load_b32(mkrs((const void*)(pc & ~4095ull), PRETOUCH_PAGES * 4096),
                                                               lane < PRETOUCH_PAGES ? ((wg >> 3) * PRETOUCH_PAGES + lane) * 128 : OOB, 0, 0);
    SB();
    const u16 *x, *b2;
    u16 *out, *dummy;
    unsigned char* ws;
    ARGS3(x, A_X, b2, A_B2, ws, A_WS);
    ARGS2(out, A_OUT, dummy, A_OUT);
    BAR0();
    const rsrc_t rs = mkrs(ws, WS_END);
    const unsigned xv = *(const GAS u16*)(x + bf * D + n);
    const unsigned b2v = *(const GAS u16*)(b2 + n);
    const unsigned tag = next_tag(*(const __attribute__((address_space(4))) unsigned*)(ws + WS_EPOCH));
    if (lane == 0) *(volatile LAS v2u*)(L + L_TG) = v2u{1u, tag};
    STAMP(22);
    __builtin_amdgcn_s_sleep(QDELAY);
    asm volatile("" :: "v"(pre));
    {
      int qo[8];
      v4u qv[8];
#pragma unroll
      for (int k = 0; k < 8; ++k) { qo[k] = WS_QP + (ab * 8 + kv) * 8192 + k * 1024 + lane * 16; qv[k] = v4u{0u, 0u, 0u, 0u}; }
      STAMP(21);
      poll<8>(rs, qo, tag, qv);
      const int col = g * 64 + 8 * (c & 7) + 4 * (c >> 3);
#pragma unroll
      for (int hp = 0; hp < 2; ++hp) {
        const f32x4 q4 = (unval(qv[hp]) + unval(qv[2 + hp])) + (unval(qv[4 + hp]) + unval(qv[6 + hp]));
        *(volatile LAS v2u*)(L + L_Q + hp * 512 + col * 2) = v2u{pk(q4[0], q4[1]), pk(q4[2], q4[3])};
      }
      if (lane == 0) *(volatile LAS v2u*)(L + L_FQ) = v2u{1u, tag};
    }
    STAMP(9);
    lds_wait4(L + L_FO, 1);
    int mo[1] = {lane < 56 ? WS_MP + wg * 1024 + lane * 16 : OOB};
    v4u mv[1] = {v4u{0u, 0u, 0u, 0u}};
    poll<1>(rs, mo, tag, mv);
    const float m = rb(rb(red4(unval(mv[0]))) + bflo(b2v));
    const float gt = rb(m * __builtin_amdgcn_rcpf(1.f + __builtin_amdgcn_exp2f(-1.4426950408889634f * m)));
    int so[2] = {WS_SP + wg * 2048 + lane * 16, WS_SP + wg * 2048 + 1024 + lane * 16};
    v4u sv[2] = {v4u{0u, 0u, 0u, 0u}, v4u{0u, 0u, 0u, 0u}};
    STAMP(14);
#pragma nounroll
    for (unsigned spin = 0; spin < SPIN_CAP * 64; ++spin) {
      if (__builtin_amdgcn_readfirstlane(*(volatile LAS unsigned*)(L + L_FS)) != SENT) break;
      __builtin_amdgcn_s_sleep(1);
    }
    v4u pa[2], pb[2];
    poll2<2>(rs, so, tag, sv, pa, pb);
    const float s = rb(red4(unval(sv[0]) + unval(sv[1])));
    const unsigned ov = pk(bflo(xv) + rb(s * gt), 0.f);
    if ((lane & 14) == 0) *(GAS u16*)(out + bf * D + n) = (u16)ov;
    if (wg == 0 && lane == 0) __builtin_amdgcn_raw_buffer_store_b32(tag, rs, WS_EPOCH, 0, 16);
    STAMP(15);
    SB();
    keep<2>(pa); keep<2>(pb);
    return;
  }
  {
    const int kw = wave - 3;
    int voff = ((ab * 8 + kv) * SK + (SK - WIN) + sp * 256 + kw * 64) * DH * 2 + lane * 16;
    asm volatile("" : "+v"(voff));
    const u16 *kc, *vc;
    unsigned char* ws;
    ARGS3(kc, A_KC, vc, A_VC, ws, A_WS);
    if (kw == 0) STAMP(0);
    const rsrc_t rs = mkrs(ws, WS_END), rk_ = mkrs(kc, 0x7fffffff), rv_ = mkrs(vc, 0x7fffffff);
    v4u R[16], V[16];
    LAS unsigned char* Kimg = L + L_K + kw * 16384;
#pragma unroll
    for (int i = 0; i < 32; ++i) {
      if (i == EARLYK) BAR0();
      if (i >= KW && i < UNTH) { asm volatile("s_waitcnt vmcnt(%0)" :: "i"(KW - 1) : "memory"); SB(); }
      if (i < 16) R[i] = ld_nt(rk_, voff + (i & 3) * 1024, (i >> 2) * 4096);
      else V[i - 16] = ld_nt(rv_, voff + (i & 3) * 1024, ((i - 16) >> 2) * 4096);
      SB();
      if (i >= KW && i - KW < 16) {
        const int k = i - KW, sig = (g << 2) | (k & 3);
        *(LAS v4u*)(Kimg + (4 * k + g) * 256 + ((c ^ sig) << 4)) = R[k];
        SB();
      }
    }
    if (lane == 0) *(volatile LAS unsigned*)(L + L_FV + kw * 4) = 1u;
    if (kw == 0) STAMP(1);
    const int keyl = 4 * (c & 3) + (c >> 2);
#pragma unroll
    for (int T = 0; T < 4; ++T)
#pragma unroll
      for (int s = 0; s < 4; ++s) R[T * 4 + s] = *(LAS v4u*)(Kimg + (16 * T + keyl) * 256 + (((4 * s + g) ^ c) << 4));
    if (kw == 0) STAMP(2);
    const unsigned tag = lds_wait_tag(L + L_FQ);
    if (kw == 0) STAMP(3);
    v4u Q[4];
#pragma unroll
    for (int s = 0; s < 4; ++s) Q[s] = *(LAS v4u*)(L + L_Q + (c >> 2) * 256 + s * 64 + g * 16);
    float sc[16];
    float mx = -INFINITY;
#pragma unroll
    for (int T = 0; T < 4; ++T) {
      f32x4 a = {0.f, 0.f, 0.f, 0.f};
#pragma unroll
      for (int s = 0; s < 4; ++s) a = mfma(R[T * 4 + s], Q[s], a);
#pragma unroll
      for (int r = 0; r < 4; r += 2) {
        const unsigned u = pk(a[r], a[r + 1]);
        const unsigned w = pk(bflo(u) * 0.08838834764831845f, bfhi(u) * 0.08838834764831845f);
        sc[T * 4 + r] = bflo(w); sc[T * 4 + r + 1] = bfhi(w);
        mx = fmaxf(mx, fmaxf(bflo(w), bfhi(w)));
      }
    }
    mx = xrow_max(mx);
    if (lane < 16 && (lane & 3) == 0) *(volatile LAS float*)(L + L_MX + (lane >> 2) * 16 + kw * 4) = mx;
    const f32x4 m4 = __builtin_bit_cast(f32x4, lds_wait4(L + L_MX + (c >> 2) * 16, 0));
    const float M = fmaxf(fmaxf(m4[0], m4[1]), fmaxf(m4[2], m4[3]));
    float ls = 0.f;
#pragma unroll
    for (int i = 0; i < 16; ++i) { sc[i] = __builtin_amdgcn_exp2f((sc[i] - M) * 1.4426950408889634f); ls += sc[i]; }
    ls = xrow_sum(ls);
    if (lane < 16 && (lane & 3) == 0) *(volatile LAS float*)(L + L_LS + (lane >> 2) * 16 + kw * 4) = ls;
    const v4u P0 = {pk(sc[0], sc[1]), pk(sc[2], sc[3]), pk(sc[4], sc[5]), pk(sc[6], sc[7])};
    const v4u P1 = {pk(sc[8], sc[9]), pk(sc[10], sc[11]), pk(sc[12], sc[13]), pk(sc[14], sc[15])};
    const int h = kv * 4 + kw;
    {
      const f32x4 l4 = __builtin_bit_cast(f32x4, lds_wait4(L + L_LS + (c >> 2) * 16, 0));
      st_unit(rs, lane == kw * 4 ? WS_ST + ((h * 4 + ab) * 8 + sp) * 16 : OOB, M, (l4[0] + l4[1]) + (l4[2] + l4[3]), 0.f, 0.f, tag);
    }
    const int ooff = lane < 32 ? WS_OP + (((h * 2 + g) * 8 + sp) * 4 + ab) * 256 + c * 16 : OOB;
    LAS unsigned char* ow = L + L_O + kw * 2048 + g * 512 + c * 32;
    LAS unsigned char* orr = L + L_O + kw * 512 + (lane & 31) * 16;
    if (kw == 0) STAMP(4);
    SB();
    f32x4 acc[8];
#pragma unroll
    for (int j = 0; j < 8; ++j) acc[j] = mfma(P0, trcol<0>(V, j), f32x4{0.f, 0.f, 0.f, 0.f});
    SB();
    {
      v4u Fv[8];
#pragma unroll
      for (int t = 0; t < 4; ++t) {
#pragma unroll
        for (int j = 0; j < 8; ++j) Fv[j][t] = __builtin_amdgcn_perm(V[8 + 2 * t + 1][j >> 1], V[8 + 2 * t][j >> 1], (j & 1) ? 0x07060302u : 0x05040100u);
        if (t < 3) SB();
      }
#pragma unroll
      for (int j = 0; j < 8; ++j) acc[j] = mfma(P1, Fv[j], acc[j]);
    }
    if (kw == 0) STAMP(5);
    ((volatile LAS f32x4*)ow)[0] = f32x4{acc[0][0], acc[1][0], acc[2][0], acc[3][0]};
    ((volatile LAS f32x4*)ow)[1] = f32x4{acc[4][0], acc[5][0], acc[6][0], acc[7][0]};
    if (lane == 0) *(volatile LAS unsigned*)(L + L_FO + kw * 4) = 1u;
    f32x4 o;
#pragma nounroll
    for (unsigned spin = 0; spin < SPIN_CAP * 64; ++spin) {
      const v4u fl = *(volatile LAS v4u*)(L + L_FO);
      const f32x4 t0 = *(volatile LAS f32x4*)(orr), t1 = *(volatile LAS f32x4*)(orr + 2048), t2 = *(volatile LAS f32x4*)(orr + 4096), t3 = *(volatile LAS f32x4*)(orr + 6144);
      o = (t0 + t1) + (t2 + t3);
      if (__builtin_amdgcn_readfirstlane(fl.x | fl.y | fl.z | fl.w) == 1u) break;
    }
    if (kw == 0) STAMP(6);
    st_unit(rs, ooff, o[0], o[1], o[2], o[3], tag);
    if (kw == 0) STAMP(7);
  }

}

extern "C" void kernel_launch(void* const* d_in, const int* in_sizes, int n_in, void* d_out, int out_size, void* d_ws,
                              size_t ws_size, hipStream_t stream) {
  fused_decode<<<NWG, NTHR, 0, stream>>>((const u16*)d_in[0], (const u16*)d_in[1], (const u16*)d_in[2], (const u16*)d_in[3],
                                         (const u16*)d_in[4], (const u16*)d_in[6], (const u16*)d_in[7], (const u16*)d_in[8],
                                         (const u16*)d_in[9], (u16*)d_out, (unsigned char*)d_ws, (unsigned char*)d_ws);
}
```

```cpp
#define EARLYK 3
#define TFIRST 150
#define TQ 340
#define THP 450
#include <hip/hip_runtime.h>
#include <stdint.h>

typedef unsigned short u16;
typedef unsigned v4u __attribute__((ext_vector_type(4)));
typedef unsigned v2u __attribute__((ext_vector_type(2)));
typedef float f32x4 __attribute__((ext_vector_type(4)));
typedef float f32x2 __attribute__((ext_vector_type(2)));
typedef __bf16 bf16x8 __attribute__((ext_vector_type(8)));
typedef __bf16 bf16x2 __attribute__((ext_vector_type(2)));
#define GAS __attribute__((address_space(1)))
#define LAS __attribute__((address_space(3)))

#define QDELAY 80
#define HDELAY 64
#define PACE 4
#define PRETOUCH_PAGES 6
#define KW 7
#define MAPKV 1
#define LATEW1 0
#define WSOFF 0
#define LATEHP 1
#define TWO (TFIRST + ((32 - EARLYK) * DELTA4) / 4)
#define TW2 (TWO + DELTA4)
#define DELTA4 58
#define KD 7
#define KW0 2
#define ADAPT 0
#define VW 9
#define VSTART (16 + KW - 3)
#define LSTART 16

constexpr int D = 512, NQ = 4096, SK = 8192, WIN = 2048, DH = 128, F = 1792;
constexpr int NWG = 256, NTHR = 704;
constexpr int WS_EPOCH = 0;
constexpr int WS_QP = 4096;
constexpr int WS_HP = WS_QP + 4 * 8 * 4 * 8 * 256;
constexpr int WS_OP = WS_HP + 28 * 8 * 1024;
constexpr int WS_ST = WS_OP + 32 * 2 * 8 * 1024;
constexpr int WS_SP = WS_ST + 32 * 4 * 8 * 16;
constexpr int WS_MP = WS_SP + 256 * 64 * 32;
constexpr int WS_END = WS_MP + 256 * 32 * 32;
static_assert(WS_END <= (2 << 20), "hand-off area");
constexpr unsigned SPIN_CAP = 1u << 16;
constexpr int OOB = 0x7ffffff0;
constexpr unsigned SENT = 0xffffffffu;

#define STAMP(i) do {} while (0)
#define STAMP_END() do {} while (0)

constexpr int L_K = 0;
constexpr int L_O = 65536;
constexpr int L_P = L_O + 8192;
constexpr int L_Q = L_P + 8192;
constexpr int L_FL = L_Q + 1024;
constexpr int L_MX = L_FL;
constexpr int L_LS = L_FL + 64;
constexpr int L_FO = L_FL + 128;
constexpr int L_FV = L_FL + 144;
constexpr int L_FQ = L_FL + 160;
constexpr int L_FS = L_FL + 176;
constexpr int L_TG = L_FL + 192;
constexpr int L_FK = L_FL + 208;
constexpr int L_FP = L_FL + 224;
constexpr int L_FF = L_FL + 240;
constexpr int L_STAMP = L_FL + 256;
constexpr int L_BYTES = L_STAMP + 256;

__device__ __forceinline__ float bflo(unsigned u) { return __builtin_bit_cast(float, u << 16); }
__device__ __forceinline__ float bfhi(unsigned u) { return __builtin_bit_cast(float, u & 0xffff0000u); }
__device__ __forceinline__ unsigned pk(float lo, float hi) {
  f32x2 v = {lo, hi};
  return __builtin_bit_cast(unsigned, __builtin_convertvector(v, bf16x2));
}
__device__ __forceinline__ float rb(float f) { return bflo(pk(f, f)); }
__device__ __forceinline__ f32x4 mfma(v4u a, v4u b, f32x4 c) {
  return __builtin_amdgcn_mfma_f32_16x16x32_bf16(__builtin_bit_cast(bf16x8, a), __builtin_bit_cast(bf16x8, b), c, 0, 0, 0);
}
template <int CTRL> __device__ __forceinline__ float dpp(float x) {
  return __builtin_bit_cast(float, __builtin_amdgcn_mov_dpp(__builtin_bit_cast(int, x), CTRL, 0xf, 0xf, true));
}
constexpr int ROR1 = 0x121, ROR2 = 0x122, ROR4 = 0x124, ROR8 = 0x128;
__device__ __forceinline__ float row_sum(float x) { x += dpp<ROR8>(x); x += dpp<ROR4>(x); x += dpp<ROR2>(x); x += dpp<ROR1>(x); return x; }
__device__ __forceinline__ float xrow_max(float x) {
  auto s = __builtin_amdgcn_permlane16_swap(__float_as_uint(x), __float_as_uint(x), false, false);
  x = fmaxf(__uint_as_float(s[0]), __uint_as_float(s[1]));
  auto t = __builtin_amdgcn_permlane32_swap(__float_as_uint(x), __float_as_uint(x), false, false);
  return fmaxf(__uint_as_float(t[0]), __uint_as_float(t[1]));
}
__device__ __forceinline__ float xrow_sum(float x) {
  auto s = __builtin_amdgcn_permlane16_swap(__float_as_uint(x), __float_as_uint(x), false, false);
  x = __uint_as_float(s[0]) + __uint_as_float(s[1]);
  auto t = __builtin_amdgcn_permlane32_swap(__float_as_uint(x), __float_as_uint(x), false, false);
  return __uint_as_float(t[0]) + __uint_as_float(t[1]);
}
__device__ __forceinline__ float red4(f32x4 v) {
  auto a = __builtin_amdgcn_permlane32_swap(__float_as_uint(v[0]), __float_as_uint(v[2]), false, false);
  auto b = __builtin_amdgcn_permlane32_swap(__float_as_uint(v[1]), __float_as_uint(v[3]), false, false);
  const float p = __uint_as_float(a[0]) + __uint_as_float(a[1]), q = __uint_as_float(b[0]) + __uint_as_float(b[1]);
  auto c = __builtin_amdgcn_permlane16_swap(__float_as_uint(p), __float_as_uint(q), false, false);
  float r = __uint_as_float(c[0]) + __uint_as_float(c[1]);
  r += dpp<ROR8>(r); r += dpp<ROR4>(r); r += dpp<ROR2>(r);
  return r;
}
template <int O, int N> __device__ __forceinline__ v4u trcol(const v4u (&W)[N], int j) {
  const unsigned sel = (j & 1) ? 0x07060302u : 0x05040100u;
  v4u r;
#pragma unroll
  for (int t = 0; t < 4; ++t) r[t] = __builtin_amdgcn_perm(W[O + 2 * t + 1][j >> 1], W[O + 2 * t][j >> 1], sel);
  return r;
}
__device__ __forceinline__ unsigned bperm(int src_lane, unsigned v) { return (unsigned)__builtin_amdgcn_ds_bpermute(src_lane << 2, (int)v); }
#define SB() __builtin_amdgcn_sched_barrier(0)
#define A_X "s[80:81]"
#define A_KC "s[82:83]"
#define A_VC "s[84:85]"
#define A_WQ "s[86:87]"
#define A_WO "s[88:89]"
#define A_W1 "s[90:91]"
#define A_B1 "s[92:93]"
#define A_W2 "s[94:95]"
#define A_B2 "s[72:73]"
#define A_OUT "s[74:75]"
#define A_WS "s[76:77]"
#define ARGS2W(p0, a0, p1, a1) asm volatile("s_waitcnt lgkmcnt(0)\n\ts_mov_b64 %0, " a0 "\n\ts_mov_b64 %1, " a1 "\n\ts_mov_b32 %2, s96" : "=&s"(p0), "=&s"(p1), "=&s"(t0) :: "memory")
#define ARGS2(p0, a0, p1, a1) asm volatile("s_mov_b64 %0, " a0 "\n\ts_mov_b64 %1, " a1 : "=&s"(p0), "=&s"(p1))
#define ARGS1(p0, a0) asm volatile("s_mov_b64 %0, " a0 : "=&s"(p0))
#define BAR0() do { SB(); asm volatile("s_barrier" ::: "memory"); SB(); } while (0)
#define VMWAIT(n) do { asm volatile("s_waitcnt vmcnt(%0)" :: "i"(n) : "memory"); SB(); } while (0)
typedef __amdgpu_buffer_rsrc_t rsrc_t;
__device__ __forceinline__ v4u ld_sc1(rsrc_t rs, int off) { return __builtin_amdgcn_raw_buffer_load_b128(rs, off, 0, 16); }
__device__ __forceinline__ v4u ld_nt(rsrc_t rs, int off, int soff) { return __builtin_amdgcn_raw_buffer_load_b128(rs, off, soff, 2); }
__device__ __forceinline__ rsrc_t mkrs(const void* p, int bytes) { return __builtin_amdgcn_make_buffer_rsrc((void*)p, 0, bytes, 0x00020000); }
__device__ __forceinline__ unsigned next_tag(unsigned e) { return ((e & 0x7f7f7f7fu) + 0x01010101u) ^ (e & 0x80808080u); }
__device__ __forceinline__ void st_unit(rsrc_t rs, int off, float a, float b, float c, float d, unsigned tag) {
  v4u v;
  v.x = __builtin_amdgcn_perm(__float_as_uint(a) + 0x80u, tag, 0x07060500u);
  v.y = __builtin_amdgcn_perm(__float_as_uint(b) + 0x80u, tag, 0x07060501u);
  v.z = __builtin_amdgcn_perm(__float_as_uint(c) + 0x80u, tag, 0x07060502u);
  v.w = __builtin_amdgcn_perm(__float_as_uint(d) + 0x80u, tag, 0x07060503u);
  __builtin_amdgcn_raw_buffer_store_b128(v, rs, off, 0, 16);
}
template <int N> __device__ __forceinline__ void pin(v4u (&a)[N]) {
#pragma unroll
  for (int k = 0; k < N; ++k) asm volatile("" : "+v"(a[k]));
}
template <int N, int NAP> __device__ __forceinline__ void poll(rsrc_t rs, int off, unsigned tag, v4u (&v)[N], unsigned long long lanes = ~0ull, int off8 = 0) {
  unsigned miss = (1u << N) - 1u;
#pragma nounroll
  for (unsigned spin = 0; spin < SPIN_CAP; ++spin) {
    asm volatile("" ::: "memory");
#pragma unroll
    for (int k = 0; k < N; ++k)
      if (miss & (1u << k)) v[k] = k < 8 ? __builtin_amdgcn_raw_buffer_load_b128(rs, off + (k & 3) * 1024, (k >> 2) * 4096, 16) : ld_sc1(rs, off8);
    asm volatile("" : "+s"(miss));
#pragma unroll
    for (int k = 0; k < N; ++k)
      if (miss & (1u << k)) {
        const unsigned t = __builtin_amdgcn_perm(v[k].y, v[k].x, 0x0c0c0400u) | __builtin_amdgcn_perm(v[k].w, v[k].z, 0x04000c0cu);
        if ((__builtin_amdgcn_ballot_w64(t != tag) & lanes) == 0ull) miss &= ~(1u << k);
      }
    if (miss == 0u) break;
    if (NAP) __builtin_amdgcn_s_sleep(NAP);
  }
}
__device__ __forceinline__ f32x4 unval(v4u v) { return __builtin_bit_cast(f32x4, v & 0xffffff00u); }
__device__ __forceinline__ v4u lds_wait4(LAS unsigned char* p, int nap) {
  v4u v;
#pragma nounroll
  for (unsigned spin = 0; spin < SPIN_CAP * 64; ++spin) {
    v = *(volatile LAS v4u*)p;
    const bool bad = v.x == SENT || v.y == SENT || v.z == SENT || v.w == SENT;
    if (__builtin_amdgcn_ballot_w64(bad) == 0ull) break;
    if (nap) __builtin_amdgcn_s_sleep(2);
  }
  asm volatile("" ::: "memory");
  return v;
}
template <int NAP> __device__ __forceinline__ void lds_wait1(LAS unsigned char* p) {
#pragma nounroll
  for (unsigned spin = 0; spin < SPIN_CAP * 64; ++spin) {
    if (__builtin_amdgcn_readfirstlane(*(volatile LAS unsigned*)p) != SENT) break;
    __builtin_amdgcn_s_sleep(NAP);
  }
  asm volatile("" ::: "memory");
}
__device__ __forceinline__ unsigned lds_wait_tag(LAS unsigned char* p) {
  v2u fq;
#pragma nounroll
  for (unsigned spin = 0; spin < SPIN_CAP * 64; ++spin) {
    fq = *(volatile LAS v2u*)p;
    if (__builtin_amdgcn_readfirstlane(fq.x) != SENT) break;
    __builtin_amdgcn_s_sleep(1);
  }
  asm volatile("" ::: "memory");
  return __builtin_amdgcn_readfirstlane(fq.y);
}
__device__ __forceinline__ void pace_until(unsigned t) {
#pragma nounroll
  while ((int)((unsigned)__builtin_amdgcn_s_memrealtime() - t) < 0) __builtin_amdgcn_s_sleep(0);
  SB();
}
__device__ __forceinline__ void nap_until(unsigned t) {
#pragma nounroll
  while ((int)((unsigned)__builtin_amdgcn_s_memrealtime() - t) < 0) __builtin_amdgcn_s_sleep(2);
  SB();
}
#define LDS_FLAG(p) do { *(volatile LAS unsigned*)(p) = 1u; } while (0)
__device__ __forceinline__ void ln_stats(const v4u (&r)[4], float& mb, float& rden) {
  f32x2 v[16];
#pragma unroll
  for (int t = 0; t < 4; ++t)
#pragma unroll
    for (int k = 0; k < 4; ++k) v[t * 4 + k] = f32x2{bflo(r[t][k]), bfhi(r[t][k])};
  f32x2 s = v[0];
#pragma unroll
  for (int i = 1; i < 16; ++i) s += v[i];
  const float mean = row_sum(s[0] + s[1]) * (1.f / D);
  f32x2 q = {0.f, 0.f};
#pragma unroll
  for (int i = 0; i < 16; ++i) { const f32x2 d = v[i] - mean; q += d * d; }
  const float var = row_sum(q[0] + q[1]) * (1.f / D);
  mb = rb(mean);
  rden = __builtin_amdgcn_rcpf(rb(__builtin_amdgcn_sqrtf(rb(rb(var) + 1e-5f))));
}
__device__ __forceinline__ unsigned ln_pair(unsigned u, float mb, float rden) {
  const unsigned d = pk(bflo(u) - mb, bfhi(u) - mb);
  return pk(bflo(d) * rden, bfhi(d) * rden);
}
__device__ __forceinline__ f32x4 hh_combine(const f32x4 (&acc)[8], bool hh) {
  f32x4 o;
#pragma unroll
  for (int t = 0; t < 4; ++t) {
    const float a0 = acc[t][0], a1 = acc[t][1], b0 = acc[4 + t][0], b1 = acc[4 + t][1];
    const float send = hh ? a1 : b0, keep = hh ? b1 : a0;
    o[t] = keep + dpp<ROR8>(send);
  }
  return o;
}

__global__ void __launch_bounds__(NTHR, 3) __attribute__((aligned(4096)))
fused_decode(const u16*, const u16*, const u16*, const u16*, const u16*, const u16*, const u16*, const u16*, const u16*, u16*,
             unsigned char*, unsigned char*) {
  asm volatile("s_memrealtime s[96:97]\n\ts_load_dwordx16 s[80:95], %0, 0x0\n\ts_load_dwordx8 s[72:79], %0, 0x40" :: "s"(__builtin_amdgcn_kernarg_segment_ptr())
               : "s72", "s73", "s74", "s75", "s76", "s77", "s78", "s79", "s80", "s81", "s82", "s83", "s84", "s85", "s86", "s87", "s88", "s89",
                 "s90", "s91", "s92", "s93", "s94", "s95", "s96", "s97");
  SB();
  unsigned long long pc;
  asm volatile("s_getpc_b64 %0" : "=s"(pc));
  unsigned t0;
  __shared__ __attribute__((aligned(16))) unsigned char lds[L_BYTES];
  LAS unsigned char* L = (LAS unsigned char*)lds;
  const int wg = blockIdx.x, lane = threadIdx.x & 63;
  const int wave = __builtin_amdgcn_readfirstlane(threadIdx.x >> 6);
  const int g = lane >> 4, c = lane & 15;
  const int kv = wg & 7, sp = (wg >> 3) & 7, ab = wg >> 6;
  const int rowl = 32 * (c >> 3) + 8 * g;
  *(volatile LAS unsigned*)(L + L_FL + lane * 4) = SENT;

  if (wave == 8) {
    __builtin_amdgcn_s_setprio(3);
    const int cb = wg & 63, rr = wg >> 6;
    int voff = ((rr * 128 + rowl) * NQ + cb * 64 + (c & 7) * 8) * 2, xoff = g * (D * 2) + c * 16;
    asm volatile("" : "+v"(voff), "+v"(xoff));
    const u16 *x, *wq, *wo;
    unsigned char* ws;
    ARGS2W(x, A_X, wq, A_WQ);
    ARGS2(wo, A_WO, ws, A_WS);
    const rsrc_t rs = mkrs(ws, WS_END);
    v4u R[16], xr[4], xo;
    {
      const rsrc_t rx_ = mkrs(x, 4 * D * 2), rq_ = mkrs(wq, D * NQ * 2);
#pragma unroll
      for (int t = 0; t < 4; ++t) xr[t] = __builtin_amdgcn_raw_buffer_load_b128(rx_, xoff + t * 256, 0, 0);
      xo = __builtin_amdgcn_raw_buffer_load_b128(rx_, xoff, rr * 256, 0);
      SB();
#pragma unroll
      for (int i = 0; i < 16; ++i) { R[i] = ld_nt(rq_, voff, ((i & 7) + (i >> 3) * 64) * (NQ * 2)); if ((i & 3) == 3) SB(); }
    }
    BAR0();
    float mb, rden;
    STAMP(16);
    ln_stats(xr, mb, rden);
    unsigned tag;
    {
      v4u A0, A1;
      const int src = (c >> 2) * 16 + 4 * (c & 1) + g;
#pragma unroll
      for (int t = 0; t < 4; ++t) { const unsigned n = ln_pair(xo[t], mb, rden); A0[t] = bperm(src, n); A1[t] = bperm(src + 8, n); }
      tag = lds_wait_tag(L + L_TG);
      STAMP(17);
      SB();
      f32x4 acc[8];
      v4u Fq[8];
#pragma unroll
      for (int j = 0; j < 8; ++j) Fq[j] = trcol<0>(R, j);
      SB();
#pragma unroll
      for (int j = 0; j < 8; ++j) acc[j] = mfma(A0, Fq[j], f32x4{0.f, 0.f, 0.f, 0.f});
      SB();
#pragma unroll
      for (int j = 0; j < 8; ++j) Fq[j] = trcol<8>(R, j);
      SB();
#pragma unroll
      for (int j = 0; j < 8; ++j) acc[j] = mfma(A1, Fq[j], acc[j]);
      SB();
      const f32x4 o = hh_combine(acc, c >= 8);
      st_unit(rs, WS_QP + (((g * 8 + (cb >> 3)) * 4 + rr) * 8 + (cb & 7)) * 256 + c * 16, o[0], o[1], o[2], o[3], tag);
    }
    LDS_FLAG(L + L_FF);
    STAMP(8);
    const int h = wg & 31, dh = (wg >> 5) & 1, nq = wg >> 6;
    __builtin_amdgcn_s_setprio(0);
    nap_until(t0 + TWO);
    STAMP(24);
    {
      const rsrc_t rw_ = mkrs(wo, NQ * D * 2);
      const int off = ((h * 128 + dh * 64 + 8 * g) * D + nq * 128 + c * 8) * 2;
#pragma unroll
      for (int i = 0; i < 16; ++i) {
        if ((i & 3) == 0 && i) pace_until(t0 + TWO + (i * DELTA4) / 16);
        R[i] = ld_nt(rw_, off + (i & 3) * 1024, ((i >> 2) & 1) * 4096 + (i >> 3) * 32768);
        if ((i & 3) == 3) SB();
      }
    }
    STAMP(25);
    v4u F0[8], F1[8];
#pragma unroll
    for (int j = 0; j < 8; ++j) F0[j] = trcol<0>(R, j);
    pin<8>(F0);
#pragma unroll
    for (int j = 0; j < 8; ++j) F1[j] = trcol<8>(R, j);
    pin<8>(F1);
    SB();
    STAMP(18);
    int src = ((c >> 2) * 16 + 2 * g) * 4;
    int spo = WS_SP + (((g * 4 + nq) * 2) * 8 + (c >> 1)) * 2048 + (h * 2 + dh) * 32 + (c & 1) * 16;
    int sto = WS_ST + ((h * 4 + g) * 8 + (c & 7)) * 16, opo = WS_OP + (h * 2 + dh) * 8192 + lane * 16;
    asm volatile("" : "+v"(src), "+v"(spo), "+v"(sto), "+v"(opo));
    lds_wait4(L + L_FO, 1);
    __builtin_amdgcn_s_setprio(3);
    STAMP(19);
    v4u ov[9];
    poll<9, 0>(rs, opo, tag, ov, ~0ull, sto);
    STAMP(10);
    const f32x4 st4 = unval(ov[8]);
    const float Ms = st4[0], Ls = st4[1];
    float Mx = fmaxf(Ms, dpp<ROR4>(Ms)); Mx = fmaxf(Mx, dpp<ROR2>(Mx)); Mx = fmaxf(Mx, dpp<ROR1>(Mx));
    const float wgt = __builtin_amdgcn_exp2f((Ms - Mx) * 1.4426950408889634f);
    float Lt = wgt * Ls; Lt += dpp<ROR4>(Lt); Lt += dpp<ROR2>(Lt); Lt += dpp<ROR1>(Lt);
    const float f = wgt * __builtin_amdgcn_rcpf(Lt);
    const float fs[8] = {dpp<0x150>(f), dpp<0x151>(f), dpp<0x152>(f), dpp<0x153>(f), dpp<0x154>(f), dpp<0x155>(f), dpp<0x156>(f), dpp<0x157>(f)};
    f32x4 o = unval(ov[0]) * fs[0];
#pragma unroll
    for (int k = 1; k < 8; ++k) o += unval(ov[k]) * fs[k];
    const int p0 = (int)pk(o[0], o[1]), p1 = (int)pk(o[2], o[3]);
#define BP(a, v) (unsigned)__builtin_amdgcn_ds_bpermute(a, v)
    const v4u A0 = {BP(src, p0), BP(src, p1), BP(src + 4, p0), BP(src + 4, p1)};
    const v4u A1 = {BP(src + 32, p0), BP(src + 32, p1), BP(src + 36, p0), BP(src + 36, p1)};
#undef BP
    f32x4 acc[8];
#pragma unroll
    for (int j = 0; j < 4; ++j) acc[j] = mfma(A0, F0[j], f32x4{0.f, 0.f, 0.f, 0.f});
#pragma unroll
    for (int j = 0; j < 4; ++j) acc[j] = mfma(A1, F1[j], acc[j]);
#pragma unroll
    for (int j = 4; j < 8; ++j) acc[j] = mfma(A0, F0[j], f32x4{0.f, 0.f, 0.f, 0.f});
#pragma unroll
    for (int j = 4; j < 8; ++j) acc[j] = mfma(A1, F1[j], acc[j]);
    SB();
    st_unit(rs, spo, acc[0][0], acc[1][0], acc[2][0], acc[3][0], tag);
    st_unit(rs, spo + 8 * 2048, acc[4][0], acc[5][0], acc[6][0], acc[7][0], tag);
    LDS_FLAG(L + L_FS);
    STAMP(11);
    STAMP_END();
    return;
  }
  if (wave == 9) {
    const int r8 = wg & 7, fb = wg >> 3;
    const int fb2 = fb < 24 ? (wg & 7) + 8 * (fb >> 3) : 24 + ((wg & 7) >> 1), nb = fb < 24 ? (fb & 7) : (fb - 24) + 4 * (wg & 1);
    if (wg < 224) {
      __builtin_amdgcn_s_setprio(3);
      int voff = ((r8 * 64 + rowl) * F + fb * 64 + (c & 7) * 8) * 2, xoff = g * (D * 2) + c * 16, xooff = g * (D * 2) + (c & 7) * 16;
      asm volatile("" : "+v"(voff), "+v"(xoff), "+v"(xooff));
      const u16 *x, *w1, *b1, *w2;
      unsigned char* ws;
      ARGS2W(x, A_X, w1, A_W1);
      ARGS2(b1, A_B1, w2, A_W2);
      ARGS1(ws, A_WS);
      const rsrc_t rs = mkrs(ws, WS_END);
      v4u R[8], xr[4], xo;
      {
        const rsrc_t rx_ = mkrs(x, 4 * D * 2), rq_ = mkrs(w1, D * F * 2);
#pragma unroll
        for (int t = 0; t < 4; ++t) xr[t] = __builtin_amdgcn_raw_buffer_load_b128(rx_, xoff + t * 256, 0, 0);
        xo = __builtin_amdgcn_raw_buffer_load_b128(rx_, xooff, r8 * 128, 0);
        SB();
#pragma unroll
        for (int i = 0; i < 8; ++i) { R[i] = ld_nt(rq_, voff, i * (F * 2)); if ((i & 3) == 3) SB(); }
      }
      BAR0();
      float mb, rden, mb2, rden2;
      ln_stats(xr, mb, rden);
      const unsigned tag = lds_wait_tag(L + L_TG);
#pragma unroll
      for (int t = 0; t < 4; ++t)
#pragma unroll
        for (int k = 0; k < 4; ++k) xr[t][k] = ln_pair(xr[t][k], mb, rden);
      ln_stats(xr, mb2, rden2);
      const int src = (c >> 2) * 16 + 4 * (c & 1) + g;
      f32x4 acc[8];
      v4u Fh[8];
      {
        v4u A0;
#pragma unroll
        for (int t = 0; t < 4; ++t) A0[t] = bperm(src, ln_pair(ln_pair(xo[t], mb, rden), mb2, rden2));
#pragma unroll
        for (int j = 0; j < 8; ++j) Fh[j] = trcol<0>(R, j);
        SB();
#pragma unroll
        for (int j = 0; j < 8; ++j) acc[j] = mfma(A0, Fh[j], f32x4{0.f, 0.f, 0.f, 0.f});
        SB();
        const f32x4 o = hh_combine(acc, c >= 8);
        st_unit(rs, WS_HP + (fb * 8 + r8) * 1024 + lane * 16, o[0], o[1], o[2], o[3], tag);
      }
      STAMP(12);
      const v2u b1p = *(const GAS v2u*)(b1 + fb2 * 64 + 8 * (c & 7) + 4 * (c >> 3));
      __builtin_amdgcn_s_setprio(0);
      nap_until(t0 + THP);
      v4u hv[8];
      poll<8, PACE>(rs, WS_HP + fb2 * 8192 + lane * 16, tag, hv);
      nap_until(t0 + TW2);
      {
        const rsrc_t rw_ = mkrs(w2, F * D * 2);
        const int off = ((fb2 * 64 + rowl) * D + nb * 64 + (c & 7) * 8) * 2;
#pragma unroll
        for (int i = 0; i < 8; ++i) R[i] = ld_nt(rw_, off + (i & 3) * 1024, (i >> 2) * 4096);
      }
      SB();
      const f32x4 u4 = ((unval(hv[0]) + unval(hv[1])) + (unval(hv[2]) + unval(hv[3]))) + ((unval(hv[4]) + unval(hv[5])) + (unval(hv[6]) + unval(hv[7])));
      const unsigned ua = pk(u4[0], u4[1]), ub = pk(u4[2], u4[3]);
      const unsigned uc[2] = {pk(bflo(ua) + bflo(b1p.x), bfhi(ua) + bfhi(b1p.x)), pk(bflo(ub) + bflo(b1p.y), bfhi(ub) + bfhi(b1p.y))};
      float hh[4];
#pragma unroll
      for (int e = 0; e < 4; ++e) {
        const float u = (e & 1) ? bfhi(uc[e >> 1]) : bflo(uc[e >> 1]);
        const float z = 0.7978845608028654f * (u + 0.044715f * u * u * u);
        hh[e] = u * __builtin_amdgcn_rcpf(1.f + __builtin_amdgcn_exp2f(-2.885390081777927f * z));
      }
      const unsigned h0 = pk(hh[0], hh[1]), h1 = pk(hh[2], hh[3]);
      const v4u A = {bperm(src, h0), bperm(src, h1), bperm(src + 8, h0), bperm(src + 8, h1)};
      STAMP(13);
      SB();
#pragma unroll
      for (int j = 0; j < 8; ++j) Fh[j] = trcol<0>(R, j);
      SB();
#pragma unroll
      for (int j = 0; j < 8; ++j) acc[j] = mfma(A, Fh[j], f32x4{0.f, 0.f, 0.f, 0.f});
      SB();
      const f32x4 o = hh_combine(acc, c >= 8);
      const int c2 = 8 * (nb & 1) + (c & 7);
      st_unit(rs, WS_MP + (((g * 4 + (nb >> 1)) * 2 + (c >> 3)) * 8 + (c2 >> 1)) * 1024 + fb2 * 32 + (c2 & 1) * 16, o[0], o[1], o[2], o[3], tag);
      STAMP(20);
    } else {
      asm volatile("s_waitcnt lgkmcnt(0)" ::: "memory");
      BAR0();
      STAMP(12); STAMP(13); STAMP(20);
    }
    STAMP_END();
    return;
  }
  if (wave == 10) {
    __builtin_amdgcn_s_setprio(3);
    const int bf = wg >> 6;
    const int n = ((wg >> 4) & 3) * 128 + 8 * (2 * (wg & 7) + (lane & 1)) + 4 * ((wg >> 3) & 1) + ((lane >> 5) & 1) * 2 + ((lane >> 4) & 1);
    const unsigned pre = __builtin_amdgcn_raw_buffer_load_b32(mkrs((const void*)(pc & ~4095ull), PRETOUCH_PAGES * 4096),
                                                               lane < PRETOUCH_PAGES ? ((wg >> 3) * PRETOUCH_PAGES + lane) * 128 : OOB, 0, 0);
    SB();
    const u16 *x, *b2;
    u16* out;
    unsigned char* ws;
    ARGS2W(x, A_X, ws, A_WS);
    ARGS2(out, A_OUT, b2, A_B2);
    const unsigned ep = *(const __attribute__((address_space(4))) unsigned*)(ws + WS_EPOCH);
    BAR0();
    const rsrc_t rs = mkrs(ws, WS_END);
    const unsigned tag = next_tag(ep);
    *(volatile LAS v2u*)(L + L_TG) = v2u{1u, tag};
    STAMP(22);
    const unsigned xv = *(const GAS u16*)(x + bf * D + n);
    const unsigned b2v = *(const GAS u16*)(b2 + n);
    nap_until(t0 + TQ);
    asm volatile("" :: "v"(pre));
    {
      v4u qv[8];
      STAMP(21);
      poll<8, PACE>(rs, WS_QP + (ab * 8 + kv) * 8192 + lane * 16, tag, qv);
      const int col = g * 64 + 8 * (c & 7) + 4 * (c >> 3);
#pragma unroll
      for (int hp = 0; hp < 2; ++hp) {
        const f32x4 q4 = (unval(qv[hp]) + unval(qv[2 + hp])) + (unval(qv[4 + hp]) + unval(qv[6 + hp]));
        *(volatile LAS v2u*)(L + L_Q + hp * 512 + col * 2) = v2u{pk(q4[0], q4[1]), pk(q4[2], q4[3])};
      }
      LDS_FLAG(L + L_FQ);
    }
    STAMP(9);
    __builtin_amdgcn_s_setprio(0);
    lds_wait4(L + L_FO, 1);
    v4u mv[1];
    poll<1, PACE>(rs, lane < 56 ? WS_MP + wg * 1024 + lane * 16 : OOB, tag, mv, (1ull << 56) - 1ull);
    const float m = rb(rb(red4(unval(mv[0]))) + bflo(b2v));
    float gt = rb(m * __builtin_amdgcn_rcpf(1.f + __builtin_amdgcn_exp2f(-1.4426950408889634f * m)));
    asm volatile("" : "+v"(gt));
    STAMP(14);
    lds_wait1<1>(L + L_FS);
    __builtin_amdgcn_s_setprio(3);
    v4u sv[2];
    poll<2, 0>(rs, WS_SP + wg * 2048 + lane * 16, tag, sv);
    const float s = rb(red4(unval(sv[0]) + unval(sv[1])));
    const unsigned ov = pk(bflo(xv) + rb(s * gt), 0.f);
    if ((lane & 14) == 0) *(GAS u16*)(out + bf * D + n) = (u16)ov;
    if (wg == 0 && lane == 0) __builtin_amdgcn_raw_buffer_store_b32(tag, rs, WS_EPOCH, 0, 16);
    STAMP(15);
    return;
  }
  const int kw = wave & 3;
  const int h = kv * 4 + kw;
  LAS unsigned char* Kimg = L + L_K + kw * 16384;
  if (wave >= 4) {
    unsigned char *ws, *ws2;
    ARGS2W(ws, A_WS, ws2, A_WS);
    BAR0();
    const rsrc_t rs = mkrs(ws, WS_END);
    const unsigned tag = lds_wait_tag(L + L_TG);
    lds_wait1<2>(L + L_FK + kw * 4);
    v4u R[16];
    const int keyl = 4 * (c & 3) + (c >> 2);
#pragma unroll
    for (int T = 0; T < 4; ++T)
#pragma unroll
      for (int s = 0; s < 4; ++s) R[T * 4 + s] = *(LAS v4u*)(Kimg + (16 * T + keyl) * 256 + (((4 * s + g) ^ c) << 4));
    if (kw == 0) STAMP(26);
    lds_wait1<1>(L + L_FQ);
    __builtin_amdgcn_s_setprio(1);
    if (kw == 0) STAMP(3);
    v4u Q[4];
#pragma unroll
    for (int s = 0; s < 4; ++s) Q[s] = *(LAS v4u*)(L + L_Q + (c >> 2) * 256 + s * 64 + g * 16);
    f32x4 a[4];
#pragma unroll
    for (int T = 0; T < 4; ++T) a[T] = mfma(R[T * 4], Q[0], f32x4{0.f, 0.f, 0.f, 0.f});
#pragma unroll
    for (int s = 1; s < 4; ++s)
#pragma unroll
      for (int T = 0; T < 4; ++T) a[T] = mfma(R[T * 4 + s], Q[s], a[T]);
    float sc[16];
    float mx = -INFINITY;
#pragma unroll
    for (int T = 0; T < 4; ++T)
#pragma unroll
      for (int r = 0; r < 4; r += 2) {
        const unsigned u = pk(a[T][r], a[T][r + 1]);
        const unsigned w = pk(bflo(u) * 0.08838834764831845f, bfhi(u) * 0.08838834764831845f);
        sc[T * 4 + r] = bflo(w); sc[T * 4 + r + 1] = bfhi(w);
        mx = fmaxf(mx, fmaxf(bflo(w), bfhi(w)));
      }
    mx = xrow_max(mx);
    if (lane < 16 && (lane & 3) == 0) *(volatile LAS float*)(L + L_MX + (lane >> 2) * 16 + kw * 4) = mx;
    const f32x4 m4 = __builtin_bit_cast(f32x4, lds_wait4(L + L_MX + (c >> 2) * 16, 0));
    const float M = fmaxf(fmaxf(m4[0], m4[1]), fmaxf(m4[2], m4[3]));
    float ls = 0.f;
#pragma unroll
    for (int i = 0; i < 16; ++i) { sc[i] = __builtin_amdgcn_exp2f((sc[i] - M) * 1.4426950408889634f); ls += sc[i]; }
    *(volatile LAS v4u*)(L + L_P + kw * 2048 + lane * 16) = v4u{pk(sc[0], sc[1]), pk(sc[2], sc[3]), pk(sc[4], sc[5]), pk(sc[6], sc[7])};
    *(volatile LAS v4u*)(L + L_P + kw * 2048 + 1024 + lane * 16) = v4u{pk(sc[8], sc[9]), pk(sc[10], sc[11]), pk(sc[12], sc[13]), pk(sc[14], sc[15])};
    LDS_FLAG(L + L_FP + kw * 4);
    if (kw == 0) STAMP(27);
    ls = xrow_sum(ls);
    if (lane < 16 && (lane & 3) == 0) *(volatile LAS float*)(L + L_LS + (lane >> 2) * 16 + kw * 4) = ls;
    const f32x4 l4 = __builtin_bit_cast(f32x4, lds_wait4(L + L_LS + (c >> 2) * 16, 0));
    if (lane == kw * 4) st_unit(rs, WS_ST + ((h * 4 + ab) * 8 + sp) * 16, M, (l4[0] + l4[1]) + (l4[2] + l4[3]), 0.f, 0.f, tag);
    if (kw == 0) STAMP(4);
    STAMP_END();
    return;
  }
  {
    int voff = ((ab * 8 + kv) * SK + (SK - WIN) + sp * 256 + kw * 64) * DH * 2 + lane * 16;
    asm volatile("" : "+v"(voff));
    const u16 *kc, *vc;
    unsigned char* ws;
    ARGS2W(kc, A_KC, vc, A_VC);
    ARGS1(ws, A_WS);
    if (kw == 0) STAMP(0);
    __builtin_amdgcn_s_setprio(2);
    const rsrc_t rs = mkrs(ws, WS_END), rk_ = mkrs(kc, 0x7fffffff), rv_ = mkrs(vc, 0x7fffffff);
    v4u R[16], V[16];
    const int rot = (((wg >> 3) + kw * 4) & 15) << 10;
    const unsigned tb = t0 + TFIRST + (kw * DELTA4) / 16;
#pragma unroll
    for (int i = 0; i < 32; ++i) {
      if (i == EARLYK) BAR0();
      if (i >= EARLYK) pace_until(tb + ((i - EARLYK) * DELTA4) / 4);
      if (i < 16) R[i] = ld_nt(rk_, voff, (i << 10) ^ rot);
      else V[i - 16] = ld_nt(rv_, voff, ((i - 16) << 10) ^ rot);
      SB();
      if (i >= KD && i - KD < 16) {
        const int k = i - KD, sig = (g << 2) | (k & 3);
        *(volatile LAS v4u*)(Kimg + (4 * k + g) * 256 + ((c ^ sig) << 4)) = R[k];
        if (k == 15) LDS_FLAG(L + L_FK + kw * 4);
        SB();
      }
    }
    if (kw == 0) STAMP(1);
    const unsigned tag = lds_wait_tag(L + L_TG);
    int ooff = lane < 32 ? WS_OP + (((h * 2 + g) * 8 + sp) * 4 + ab) * 256 + c * 16 : OOB;
    asm volatile("" : "+v"(ooff));
    LAS unsigned char* ow = L + L_O + kw * 2048 + g * 512 + c * 32;
    LAS unsigned char* orr = L + L_O + kw * 512 + (lane & 31) * 16;
    v4u Fv[8];
#pragma unroll
    for (int j = 0; j < 8; ++j) Fv[j] = trcol<0>(V, j);
    pin<8>(Fv);
    __builtin_amdgcn_s_setprio(0);
    lds_wait1<1>(L + L_FP + kw * 4);
    __builtin_amdgcn_s_setprio(3);
    const v4u P0 = *(LAS v4u*)(L + L_P + kw * 2048 + lane * 16), P1 = *(LAS v4u*)(L + L_P + kw * 2048 + 1024 + lane * 16);
    if (kw == 0) STAMP(2);
    f32x4 acc[8];
#pragma unroll
    for (int j = 0; j < 8; ++j) acc[j] = mfma(P0, Fv[j], f32x4{0.f, 0.f, 0.f, 0.f});
    SB();
#pragma unroll
    for (int t = 0; t < 4; ++t) {
#pragma unroll
      for (int j = 0; j < 8; ++j) Fv[j][t] = __builtin_amdgcn_perm(V[8 + 2 * t + 1][j >> 1], V[8 + 2 * t][j >> 1], (j & 1) ? 0x07060302u : 0x05040100u);
      SB();
    }
#pragma unroll
    for (int j = 0; j < 8; ++j) acc[j] = mfma(P1, Fv[j], acc[j]);
    SB();
    if (kw == 0) STAMP(5);
    ((volatile LAS f32x4*)ow)[0] = f32x4{acc[0][0], acc[1][0], acc[2][0], acc[3][0]};
    ((volatile LAS f32x4*)ow)[1] = f32x4{acc[4][0], acc[5][0], acc[6][0], acc[7][0]};
    LDS_FLAG(L + L_FO + kw * 4);
    f32x4 o;
#pragma nounroll
    for (unsigned spin = 0; spin < SPIN_CAP * 64; ++spin) {
      const v4u fl = *(volatile LAS v4u*)(L + L_FO);
      const f32x4 t0 = *(volatile LAS f32x4*)(orr), t1 = *(volatile LAS f32x4*)(orr + 2048), t2 = *(volatile LAS f32x4*)(orr + 4096), t3 = *(volatile LAS f32x4*)(orr + 6144);
      o = (t0 + t1) + (t2 + t3);
      if (__builtin_amdgcn_readfirstlane(fl.x | fl.y | fl.z | fl.w) == 1u) break;
    }
    if (kw == 0) STAMP(6);
    st_unit(rs, ooff, o[0], o[1], o[2], o[3], tag);
    if (kw == 0) STAMP(7);
    STAMP_END();
  }
}

extern "C" void kernel_launch(void* const* d_in, const int* in_sizes, int n_in, void* d_out, int out_size, void* d_ws,
                              size_t ws_size, hipStream_t stream) {
  fused_decode<<<NWG, NTHR, 0, stream>>>((const u16*)d_in[0], (const u16*)d_in[1], (const u16*)d_in[2], (const u16*)d_in[3],
                                         (const u16*)d_in[4], (const u16*)d_in[6], (const u16*)d_in[7], (const u16*)d_in[8],
                                         (const u16*)d_in[9], (u16*)d_out, (unsigned char*)d_ws + WSOFF, (unsigned char*)d_ws);
}
```
